```python
import torch

def _softmax(x, axis=-1):
    return torch.softmax(x, dim=axis)
def _gelu(x):
    return torch.nn.functional.gelu(x, approximate="tanh")
def _silu(x):
    return torch.nn.functional.silu(x)
def _sigmoid(x):
    return torch.sigmoid(x)
def _layernorm(x, axis=-1, eps=1e-5):
    m = x.mean(dim=axis, keepdim=True)
    v = x.var(dim=axis, keepdim=True, unbiased=False)
    return (x - m) / torch.sqrt(v + eps)
def _scatter_add(tgt, idx, src, axis=0):
    return tgt.clone().index_add_(axis, idx, src)
def _segment_sum(src, seg, n):
    out = torch.zeros((n,) + src.shape[1:], dtype=src.dtype)
    return out.index_add_(0, seg, src)
def _pad(x, pw):
    flat = tuple(v for pair in reversed(pw) for v in pair)
    return torch.nn.functional.pad(x, flat)

kernel_name = 'gated_mix__linear_kernel_hedgehog__mixer_token_mlp__prompt'

def setup_inputs():

    _w = (lambda a: (lambda t: t.to(torch.bfloat16) if t.is_floating_point() else t)(torch.as_tensor(a)))
    def _A_setup():
        B, S, D, H = 1, 2048, 512, 32
        Dh = D // H
        return {
            "x": _w((lambda *s: torch.randn(*s, dtype=torch.bfloat16) * 0.05)(B, S, D)),
            "wqkv": _w((lambda *s: torch.randn(*s, dtype=torch.bfloat16) * 0.8)(D, 3 * D)),
            "w_phi": _w((lambda *s: torch.randn(*s, dtype=torch.bfloat16) * 0.8)(H, Dh, Dh) * (Dh ** -0.5)),
            "wo": _w((lambda *s: torch.randn(*s, dtype=torch.bfloat16) * 0.05)(D, D)),
        }
    _A_in = _A_setup()

    def _B_setup():
        B, S, D, F = 1, 2048, 512, 1792
        return {
            "x": _w((lambda *s: torch.randn(*s, dtype=torch.bfloat16) * 0.05)(B, S, D)),
            "w1": _w((lambda *s: torch.randn(*s, dtype=torch.bfloat16) * 0.4)(S, F) * (S ** -0.5)),
            "b1": _w((lambda *s: torch.randn(*s, dtype=torch.bfloat16) * 3.2)(F) * 0.02),
            "w2": _w((lambda *s: torch.randn(*s, dtype=torch.bfloat16) * 0.1)(F, S) * (F ** -0.5)),
            "b2": _w((lambda *s: torch.randn(*s, dtype=torch.bfloat16) * 0.4)(S) * 0.02),
        }
    _B_in = _B_setup()

    return {
        "A_x": _A_in["x"],
        "A_wqkv": _A_in["wqkv"],
        "A_w_phi": _A_in["w_phi"],
        "A_wo": _A_in["wo"],
        "B_x": _B_in["x"],
        "B_w1": _B_in["w1"],
        "B_b1": _B_in["b1"],
        "B_w2": _B_in["w2"],
        "B_b2": _B_in["b2"],
        "gate_w": _w((lambda *s: torch.randn(*s, dtype=torch.bfloat16) * 0.05)(512) * 16.0),
    }

def reference(A_x, A_wqkv, A_w_phi, A_wo, B_x, B_w1, B_b1, B_w2, B_b2, gate_w):
    def _A_ref(x, wqkv, w_phi, wo):
        B, S, D = x.shape
        H = 32; Dh = D // H
        qkv = (lambda x, *d: x.permute(*d))((x @ wqkv).reshape(B, S, 3, H, Dh), 2, 0, 3, 1, 4)
        q, k, v = qkv[0], qkv[1], qkv[2]
        pq = torch.einsum("bhsd,hdf->bhsf", q, w_phi)
        pk = torch.einsum("bhsd,hdf->bhsf", k, w_phi)
        phi_q = torch.concatenate([_softmax(pq, axis=-1), _softmax(-pq, axis=-1)], axis=-1)
        phi_k = torch.concatenate([_softmax(pk, axis=-1), _softmax(-pk, axis=-1)], axis=-1)
        kv = torch.einsum("bhsf,bhsd->bhfd", phi_k, v)
        z  = torch.einsum("bhsf->bhf", phi_k)
        num = torch.einsum("bhqf,bhfd->bhqd", phi_q, kv)
        den = torch.einsum("bhqf,bhf->bhq", phi_q, z)[..., None] + 1e-6
        o = num / den
        return (lambda x, *d: x.permute(*d))(o, 0, 2, 1, 3).reshape(B, S, D) @ wo

    def _B_ref(x, w1, b1, w2, b2):
        B, S, D = x.shape
        h = _layernorm(x, axis=-1)
        u = torch.einsum("bsd,sf->bdf", h, w1) + b1
        u = _gelu(u)
        y = torch.einsum("bdf,fs->bds", u, w2) + b2
        return (lambda x, *d: x.permute(*d))(y, 0, 2, 1)

    x = A_x
    g = _sigmoid((x * gate_w).sum(-1, keepdims=True))
    a = _A_ref(x, A_wqkv, A_w_phi, A_wo)
    b = _B_ref(x, B_w1, B_b1, B_w2, B_b2)
    return g * a + (1 - g) * b
```

```cpp
#include <hip/hip_runtime.h>
#include <hip/hip_bf16.h>
#include <stdint.h>

typedef __attribute__((ext_vector_type(8))) __bf16 bf8;
typedef __attribute__((ext_vector_type(4))) __bf16 bf4;
typedef __attribute__((ext_vector_type(2))) __bf16 bf2;
typedef __attribute__((ext_vector_type(2))) float f2;
typedef __attribute__((ext_vector_type(4))) float f4;
typedef __attribute__((ext_vector_type(8))) float f8;
typedef __attribute__((ext_vector_type(16))) float f16v;
typedef __attribute__((ext_vector_type(4))) unsigned u4;
typedef __attribute__((ext_vector_type(2))) unsigned u2;
typedef __attribute__((address_space(3))) char lchar;
#define LDSP(T, a) ((__attribute__((address_space(3))) T*)(a))
#define DI __device__ __forceinline__

constexpr int S_ = 2048, D_ = 512, F_ = 1792;
constexpr size_t WS_H = 0, WS_PQ = 2u << 20, WS_KV = 6u << 20, WS_G = 8u << 20, WS_U = 9u << 20, WS_O = 11u << 20;
constexpr int KVP = 544;
constexpr int LDS_BYTES = 153600;

struct Args {
  const char *x, *wqkv, *wphi, *wo, *w1, *b1, *w2, *b2, *gw;
  char *out, *ws;
};

#define MFMA(a, b, c) __builtin_amdgcn_mfma_f32_32x32x16_bf16(a, b, c, 0, 0, 0)
#define WAITV(n) asm volatile("s_waitcnt vmcnt(%0) lgkmcnt(0)" ::"i"(n) : "memory")
#define BAR() do { asm volatile("s_waitcnt lgkmcnt(0)" ::: "memory"); __builtin_amdgcn_s_barrier(); asm volatile("" ::: "memory"); } while (0)

DI void dma4(unsigned v0, unsigned v1, unsigned v2, unsigned v3, const char* base, unsigned m0) {
  asm volatile("s_mov_b32 m0, %5\n\ts_nop 0\n\tglobal_load_lds_dwordx4 %0, %4\n\tglobal_load_lds_dwordx4 %1, %4 offset:1024\n\t"
               "global_load_lds_dwordx4 %2, %4 offset:2048\n\tglobal_load_lds_dwordx4 %3, %4 offset:3072"
               ::"v"(v0), "v"(v1), "v"(v2), "v"(v3), "s"(base), "s"(m0) : "memory");
}
DI void dma3(unsigned v0, const char* base, unsigned m0) {
  asm volatile("s_mov_b32 m0, %2\n\ts_nop 0\n\tglobal_load_lds_dwordx4 %0, %1\n\tglobal_load_lds_dwordx4 %0, %1 offset:1024\n\t"
               "global_load_lds_dwordx4 %0, %1 offset:2048" ::"v"(v0), "s"(base), "s"(m0) : "memory");
}
DI void dma2(unsigned v0, const char* base, unsigned m0) {
  asm volatile("s_mov_b32 m0, %2\n\ts_nop 0\n\tglobal_load_lds_dwordx4 %0, %1\n\tglobal_load_lds_dwordx4 %0, %1 offset:1024"
               ::"v"(v0), "s"(base), "s"(m0) : "memory");
}
DI void dma1(unsigned v0, const char* base, unsigned m0) {
  asm volatile("s_mov_b32 m0, %2\n\ts_nop 0\n\tglobal_load_lds_dwordx4 %0, %1" ::"v"(v0), "s"(base), "s"(m0) : "memory");
}
DI void dma1w(unsigned v0, const char* base, unsigned m0) {
  asm volatile("s_mov_b32 m0, %2\n\ts_nop 0\n\tglobal_load_lds_dword %0, %1" ::"v"(v0), "s"(base), "s"(m0) : "memory");
}
DI bf8 trd(lchar* p0, lchar* p1) {
  bf4 a = __builtin_amdgcn_ds_read_tr16_b64_v4bf16(LDSP(bf4, p0));
  bf4 b = __builtin_amdgcn_ds_read_tr16_b64_v4bf16(LDSP(bf4, p1));
  return __builtin_shufflevector(a, b, 0, 1, 2, 3, 4, 5, 6, 7);
}
DI bf8 pack8(const f16v& c, int s) {
  f8 t = s ? __builtin_shufflevector(c, c, 8, 9, 10, 11, 12, 13, 14, 15) : __builtin_shufflevector(c, c, 0, 1, 2, 3, 4, 5, 6, 7);
  return __builtin_convertvector(t, bf8);
}
DI float rbf(float x) { return (float)(__bf16)x; }
DI float bflo(unsigned u) { return __builtin_bit_cast(float, u << 16); }
DI float bfhi(unsigned u) { return __builtin_bit_cast(float, u & 0xffff0000u); }
DI float xhalf_max(float a) { auto r = __builtin_amdgcn_permlane32_swap(__builtin_bit_cast(unsigned, a), __builtin_bit_cast(unsigned, a), false, false);
  unsigned r0 = r[0], r1 = r[1]; return fmaxf(__builtin_bit_cast(float, r0), __builtin_bit_cast(float, r1)); }
DI float xhalf_add(float a) { auto r = __builtin_amdgcn_permlane32_swap(__builtin_bit_cast(unsigned, a), __builtin_bit_cast(unsigned, a), false, false);
  unsigned r0 = r[0], r1 = r[1]; return __builtin_bit_cast(float, r0) + __builtin_bit_cast(float, r1); }
DI float wave_sum(float v) {
#pragma unroll
  for (int o = 1; o < 32; o <<= 1) v += __shfl_xor(v, o);
  return xhalf_add(v);
}
DI void dual_softmax(const f16v& c, int s, bf8& pos, bf8& neg) {
  const float L2E = 1.4426950408889634f;
  float v[8];
#pragma unroll
  for (int j = 0; j < 8; ++j) v[j] = c[8 * s + j] * L2E;
  float mx = v[0], mn = v[0];
#pragma unroll
  for (int j = 1; j < 8; ++j) { mx = fmaxf(mx, v[j]); mn = fminf(mn, v[j]); }
  mx = xhalf_max(mx); mn = -xhalf_max(-mn);
  f8 ep, en; float sp = 0.f, sn = 0.f;
#pragma unroll
  for (int j = 0; j < 8; ++j) { ep[j] = __builtin_amdgcn_exp2f(v[j] - mx); en[j] = __builtin_amdgcn_exp2f(mn - v[j]); sp += ep[j]; sn += en[j]; }
  sp = __builtin_amdgcn_rcpf(xhalf_add(sp)); sn = __builtin_amdgcn_rcpf(xhalf_add(sn));
  pos = __builtin_convertvector(ep * sp, bf8); neg = __builtin_convertvector(en * sn, bf8);
}

DI void phase1(const Args& A, int bid, lchar* sm) {
  const int tid = threadIdx.x, lane = tid & 63, w = __builtin_amdgcn_readfirstlane(tid >> 6);
  const int xcd = bid & 7, i = bid >> 3;
  const int tt = (xcd >> 1) * 4 + (i >> 3), ct = (xcd & 1) * 8 + (i & 7);
  const unsigned l0 = (unsigned)(size_t)sm;
  constexpr int SLOT = 28672, LNX = 143360, GW = 151552, WP = 152576;
  const int r = lane & 31, h = lane >> 5;
  const int tok0 = tt * 128 + ct * 8;
  dma2(lane * 16, A.x + (size_t)(tok0 + 2 * w) * 1024, l0 + LNX + w * 2048);
  dma1(lane * 16, A.gw, l0 + GW);
  dma1(lane * 16, A.wphi + ct * 1024, l0 + WP);
  unsigned vx[4];
#pragma unroll
  for (int j = 0; j < 4; ++j) { int row = 8 * j + (lane >> 3); vx[j] = row * 1024 + (((lane & 7) ^ ((row >> 1) & 7)) << 4) - j * 1024; }
  const unsigned vw = (lane >> 2) * 3072 + (lane & 3) * 16;
  const char* xb = A.x + (size_t)(tt * 128 + 32 * w) * 1024;
  const char* wb = A.wqkv + (size_t)(16 * w) * 3072 + ct * 64;
  auto issue = [&](int c) {
    const unsigned sl = l0 + (c % 5) * SLOT;
    dma4(vx[0], vx[1], vx[2], vx[3], xb + c * 128, sl + w * 4096);
    dma3(vw, wb + (size_t)c * 64 * 3072, sl + 16384 + w * 3072);
  };
#pragma unroll
  for (int c = 0; c < 5; ++c) issue(c);
  WAITV(35);
  {
    u4 gwv = *LDSP(u4, sm + GW + lane * 16);
#pragma unroll
    for (int q = 0; q < 2; ++q) {
      lchar* p = sm + LNX + (2 * w + q) * 1024 + lane * 16;
      u4 xv = *LDSP(u4, p);
      float v[8], s = 0.f, gs = 0.f;
#pragma unroll
      for (int j = 0; j < 4; ++j) { v[2 * j] = bflo(xv[j]); v[2 * j + 1] = bfhi(xv[j]);
        s += v[2 * j] + v[2 * j + 1]; gs += rbf(v[2 * j] * bflo(gwv[j])) + rbf(v[2 * j + 1] * bfhi(gwv[j])); }
      s = wave_sum(s) * (1.f / 512.f); gs = wave_sum(gs);
      float vs = 0.f;
#pragma unroll
      for (int j = 0; j < 8; ++j) { float d = v[j] - s; vs += d * d; }
      vs = wave_sum(vs) * (1.f / 512.f);
      const float m = rbf(s), sd = rbf(sqrtf(rbf(rbf(vs) + 1e-5f)));
      f8 o;
#pragma unroll
      for (int j = 0; j < 8; ++j) o[j] = rbf(v[j] - m) / sd;
      *LDSP(bf8, p) = __builtin_convertvector(o, bf8);
      if (lane == 0) ((float*)(A.ws + WS_G))[tok0 + 2 * w + q] = rbf(1.f / (1.f + __expf(-rbf(gs))));
    }
  }
  f16v aq = {}, ak = {}, av = {};
  lchar* xr = sm + w * 4096 + r * 128;
  const int xs = (r >> 1) & 7;
  lchar* wr = sm + 16384 + (8 * h + ((lane & 15) >> 2)) * 64 + ((lane >> 4) & 1) * 32 + (lane & 3) * 8;
#pragma unroll
  for (int c = 0; c < 8; ++c) {
    if (c == 0) WAITV(28); else if (c <= 4) WAITV(21); else if (c == 5) WAITV(14); else if (c == 6) WAITV(7); else WAITV(0);
    BAR();
    if (c >= 1 && c + 4 < 8) issue(c + 4);
    if (c == 0) {
      unsigned t[8];
#pragma unroll
      for (int j = 0; j < 8; ++j) t[j] = *LDSP(unsigned, sm + LNX + j * 1024 + tid * 4);
      u4 lo, hi;
#pragma unroll
      for (int j = 0; j < 4; ++j) { lo[j] = (t[2 * j] & 0xffffu) | (t[2 * j + 1] << 16); hi[j] = (t[2 * j] >> 16) | (t[2 * j + 1] & 0xffff0000u); }
      const int d = 2 * tid, dl = d & 31, rr = (dl & 19) | ((dl & 4) << 1) | ((dl & 8) >> 1);
      char* hp = A.ws + WS_H + ((size_t)((d >> 6) * 128 + (tok0 >> 4)) * 2 + ((d >> 5) & 1)) * 1024 + (((tok0 >> 3) & 1) * 32 + rr) * 16;
      *(u4*)hp = lo; *(u4*)(hp + 16) = hi;
    }
    const int sl = (c % 5) * SLOT;
#pragma unroll
    for (int ks = 0; ks < 4; ++ks) {
      bf8 xf = *LDSP(bf8, xr + sl + (((2 * ks + h) ^ xs) << 4));
      lchar* p = wr + sl + ks * 3072;
      bf8 wq = trd(p, p + 256), wk = trd(p + 1024, p + 1280), wv = trd(p + 2048, p + 2304);
      aq = MFMA(wq, xf, aq); ak = MFMA(wk, xf, ak); av = MFMA(xf, wv, av);
    }
  }
  bf8 wp[2], id[2];
#pragma unroll
  for (int s = 0; s < 2; ++s) {
#pragma unroll
    for (int j = 0; j < 8; ++j) {
      const int d = 8 * (j >> 2) + 4 * h + (j & 3);
      __bf16 v = *LDSP(__bf16, sm + WP + s * 512 + d * 32 + (r & 15) * 2);
      wp[s][j] = ((r >> 4) == s) ? v : (__bf16)0.f;
      id[s][j] = (r == 16 * s + d) ? (__bf16)1.f : (__bf16)0.f;
    }
  }
  f16v z16 = {};
  f16v pq = MFMA(wp[0], pack8(aq, 0), z16); pq = MFMA(wp[1], pack8(aq, 1), pq);
  f16v pk = MFMA(wp[0], pack8(ak, 0), z16); pk = MFMA(wp[1], pack8(ak, 1), pk);
  bf8 kp[2], kn[2];
  dual_softmax(pk, 0, kp[0], kn[0]); dual_softmax(pk, 1, kp[1], kn[1]);
  f16v zp = MFMA(kp[0], id[0], z16); zp = MFMA(kp[1], id[1], zp);
  f16v zn = MFMA(kn[0], id[0], z16); zn = MFMA(kn[1], id[1], zn);
#pragma unroll
  for (int s = 0; s < 2; ++s) {
    bf8 qp, qn; dual_softmax(pq, s, qp, qn);
    char* p = A.ws + WS_PQ + ((size_t)(2 * ct + s) * 64 + tt * 4 + w) * 2048 + lane * 16;
    *(bf8*)p = qp; *(bf8*)(p + 1024) = qn;
  }
  bf8 v0 = pack8(av, 0), v1 = pack8(av, 1);
  f16v kvp = MFMA(pack8(zp, 0), v0, z16); kvp = MFMA(pack8(zp, 1), v1, kvp);
  f16v kvn = MFMA(pack8(zn, 0), v0, z16); kvn = MFMA(pack8(zn, 1), v1, kvn);
  float sp = 0.f, sn = 0.f;
#pragma unroll
  for (int j = 0; j < 16; ++j) { sp += zp[j]; sn += zn[j]; }
  sp = xhalf_add(sp); sn = xhalf_add(sn);
  {
    const int hd = r >> 4, dd = r & 15;
    lchar* p = sm + (w * 2 + hd) * (KVP * 4) + (h * 16 + dd) * 32;
    f4 a0, a1, b0, b1;
#pragma unroll
    for (int j = 0; j < 4; ++j) { a0[j] = hd ? kvp[8 + j] : kvp[j]; a1[j] = hd ? kvp[12 + j] : kvp[4 + j];
                                  b0[j] = hd ? kvn[8 + j] : kvn[j]; b1[j] = hd ? kvn[12 + j] : kvn[4 + j]; }
    *LDSP(f4, p) = a0; *LDSP(f4, p + 16) = a1; *LDSP(f4, p + 1024) = b0; *LDSP(f4, p + 1040) = b1;
    const int zi = ((dd >> 2) & 1) * 8 + (dd >> 3) * 4 + (dd & 3);
    *LDSP(float, sm + (w * 2 + hd) * (KVP * 4) + 2048 + h * 64 + zi * 4) = h ? sn : sp;
  }
  BAR();
  for (int t = tid; t < 2 * (KVP / 4); t += 256) {
    f4 s = *LDSP(f4, sm + t * 16);
#pragma unroll
    for (int q = 1; q < 4; ++q) s += *LDSP(f4, sm + q * 2 * KVP * 4 + t * 16);
    const int hd = t >= KVP / 4, e = t - hd * (KVP / 4);
    *(f4*)(A.ws + WS_KV + ((size_t)(2 * ct + hd) * 16 + tt) * (KVP * 4) + e * 16) = s;
  }
}

struct TrOff { unsigned v[4]; };
DI TrOff tr_off(int lane, int pitch) {
  TrOff o;
#pragma unroll
  for (int j = 0; j < 4; ++j) o.v[j] = (8 * j + (lane >> 3)) * pitch + (((lane & 7) ^ ((lane >> 2) & 4)) << 4) - j * 1024;
  return o;
}
DI void reduce4(f16v (&acc)[4], lchar* sm, int w, int lane) {
  asm volatile("s_waitcnt lgkmcnt(0)" ::: "memory");
#pragma unroll
  for (int i = 1; i < 4; ++i)
#pragma unroll
    for (int q = 0; q < 4; ++q) {
      f4 t = {acc[i][4 * q], acc[i][4 * q + 1], acc[i][4 * q + 2], acc[i][4 * q + 3]};
      *LDSP(f4, sm + w * 32768 + (i * 4 + q) * 1024 + lane * 16) = t;
    }
  BAR();
#pragma unroll
  for (int i = 1; i < 4; ++i)
#pragma unroll
    for (int q = 0; q < 4; ++q) {
      f4 t = *LDSP(f4, sm + (w ^ i) * 32768 + (i * 4 + q) * 1024 + lane * 16);
#pragma unroll
      for (int j = 0; j < 4; ++j) acc[0][4 * q + j] += t[j];
    }
}

DI void o_phase(const Args& A, int head, lchar* sm) {
  const int tid = threadIdx.x, lane = tid & 63, w = __builtin_amdgcn_readfirstlane(tid >> 6);
  const int r = lane & 31, h = lane >> 5;
  f4 s = {};
  if (tid < KVP / 4) {
    f4 t[16];
#pragma unroll
    for (int q = 0; q < 16; ++q) t[q] = *(const f4*)(A.ws + WS_KV + ((size_t)head * 16 + q) * (KVP * 4) + tid * 16);
#pragma unroll
    for (int q = 0; q < 16; ++q) s += t[q];
  }
  bf8 ph[32];
  const char* pp = A.ws + WS_PQ + ((size_t)head * 64 + w * 16) * 2048 + lane * 16;
#pragma unroll
  for (int q = 0; q < 32; ++q) ph[q] = *(const bf8*)(pp + q * 1024);
  if (tid < KVP / 4) *LDSP(bf4, sm + tid * 8) = __builtin_convertvector(s, bf4);
  BAR();
  bf8 a[2];
#pragma unroll
  for (int q = 0; q < 2; ++q) {
    const int d = (r & 3) | ((r & 4) << 1) | ((r & 8) >> 1);
    const int off = r < 16 ? q * 512 + (h * 16 + d) * 16 : 1024 + q * 32 + h * 16;
    bf8 t = *LDSP(bf8, sm + off);
    bf8 zz = {};
    a[q] = (r < 16 || r == 16 || r == 20) ? t : zz;
  }
  f16v z16 = {};
#pragma unroll
  for (int q = 0; q < 16; ++q) {
    f16v c = MFMA(a[0], ph[2 * q], z16); c = MFMA(a[1], ph[2 * q + 1], c);
    const float inv = 1.f / (c[8] + 1e-6f);
    f8 o = __builtin_shufflevector(c, c, 0, 1, 2, 3, 4, 5, 6, 7) * inv;
    const int sb = w * 16 + q;
    *(bf8*)(A.ws + WS_O + ((size_t)((sb >> 1) * 32 + head) * 2 + (sb & 1)) * 1024 + lane * 16) = __builtin_convertvector(o, bf8);
  }
}

DI void phase2(const Args& A, int bid, lchar* sm) {
  const int tid = threadIdx.x, lane = tid & 63, w = __builtin_amdgcn_readfirstlane(tid >> 6);
  const int xcd = bid & 7, i = bid >> 3;
  if (i >= 28) { o_phase(A, xcd * 4 + i - 28, sm); return; }
  const int dt = (xcd & 1) * 4 + (i & 3), ft = (xcd >> 1) * 7 + (i >> 2);
  const unsigned l0 = (unsigned)(size_t)sm + w * 32768;
  const int h = lane >> 5, q = (lane & 15) >> 2;
  constexpr int BIAS = 131072;
  dma1w((lane & 31) * 4, A.b1 + ft * 128, (unsigned)(size_t)sm + BIAS);
  const TrOff to = tr_off(lane, F_ * 2);
  const unsigned vd = lane * 16;
  const char* db = A.ws + WS_H + (size_t)(dt * 128 + w * 32) * 2048;
  const char* tb = A.w1 + (size_t)(w * 512) * (F_ * 2) + ft * 128;
  auto issue = [&](int c) {
    dma4(to.v[0], to.v[1], to.v[2], to.v[3], tb + (size_t)c * 32 * (F_ * 2), l0 + (c & 3) * 8192 + 4096);
    dma4(vd, vd, vd, vd, db + c * 4096, l0 + (c & 3) * 8192);
  };
  issue(0); issue(1); issue(2);
  f16v acc[4] = {};
  lchar* dr[2]; lchar* tr[2];
#pragma unroll
  for (int b = 0; b < 2; ++b) {
    dr[b] = sm + w * 32768 + ((b ^ (w & 1)) << 10) + lane * 16;
    tr[b] = sm + w * 32768 + 4096 + (8 * h + q) * 128 + ((b ^ (w >> 1) ^ (q >> 1)) << 6) + ((lane >> 4) & 1) * 32 + (lane & 3) * 8;
  }
#pragma unroll
  for (int c = 0; c < 16; ++c) {
    if (c < 14) WAITV(16); else if (c == 14) WAITV(8); else WAITV(0);
    const int sl = (c & 3) * 8192;
    bf8 d[2][2], t[2][2];
#pragma unroll
    for (int ks = 0; ks < 2; ++ks)
#pragma unroll
      for (int b = 0; b < 2; ++b) {
        t[ks][b] = trd(tr[b] + sl + ks * 2048, tr[b] + sl + ks * 2048 + 512);
        d[ks][b] = *LDSP(bf8, dr[b] + sl + ks * 2048);
      }
    if (c + 3 < 16) issue(c + 3);
#pragma unroll
    for (int ks = 0; ks < 2; ++ks)
#pragma unroll
      for (int a = 0; a < 4; ++a) acc[a] = MFMA(t[ks][a >> 1], d[ks][a & 1], acc[a]);
  }
  reduce4(acc, sm, w, lane);
  const int fb = w >> 1, dbk = w & 1;
  f16v u;
#pragma unroll
  for (int g = 0; g < 4; ++g) {
    u2 bb = *LDSP(u2, sm + BIAS + (fb * 32 + 8 * g + 4 * h) * 2);
    float bv[4] = {bflo(bb[0]), bfhi(bb[0]), bflo(bb[1]), bfhi(bb[1])};
#pragma unroll
    for (int j = 0; j < 4; ++j) {
      const float x = acc[0][4 * g + j] + bv[j];
      const float y = x * (-2.3022081985f) * (1.f + 0.044715f * x * x);
      u[4 * g + j] = x * __builtin_amdgcn_rcpf(1.f + __builtin_amdgcn_exp2f(y));
    }
  }
  char* up = A.ws + WS_U + ((size_t)(dt * 112 + ft * 4 + fb * 2) * 2 + dbk) * 1024 + lane * 16;
  *(bf8*)up = pack8(u, 0); *(bf8*)(up + 2048) = pack8(u, 1);
}

DI void phase3(const Args& A, int bid, lchar* sm) {
  const int tid = threadIdx.x, lane = tid & 63, w = __builtin_amdgcn_readfirstlane(tid >> 6);
  const int xcd = bid & 7, i = bid >> 3;
  const int dt = (xcd & 1) * 4 + (i & 3), st = (xcd >> 1) * 8 + (i >> 2);
  const unsigned l0 = (unsigned)(size_t)sm + w * 32768;
  const int r = lane & 31, h = lane >> 5, q = (lane & 15) >> 2;
  constexpr int GG = 131072, B2 = 131072 + 256;
  dma1w(lane * 4, A.ws + WS_G + st * 256, (unsigned)(size_t)sm + GG);
  dma1w((lane & 31) * 4, A.b2 + st * 128, (unsigned)(size_t)sm + B2);
  const TrOff t2 = tr_off(lane, S_ * 2), t1 = tr_off(lane, D_ * 2);
  const unsigned vd = lane * 16;
  const char* d2 = A.ws + WS_U + (size_t)(dt * 112 + w * 28) * 2048;
  const char* b2 = A.w2 + (size_t)(w * 448) * (S_ * 2) + st * 128;
  const char* d1 = A.ws + WS_O + (size_t)(st * 32 + w * 8) * 2048;
  const char* b1 = A.wo + (size_t)(w * 128) * (D_ * 2) + dt * 128;
  auto issue = [&](int c) {
    if (c < 14) {
      dma4(t2.v[0], t2.v[1], t2.v[2], t2.v[3], b2 + (size_t)c * 32 * (S_ * 2), l0 + (c & 3) * 8192 + 4096);
      dma4(vd, vd, vd, vd, d2 + c * 4096, l0 + (c & 3) * 8192);
    } else {
      dma4(t1.v[0], t1.v[1], t1.v[2], t1.v[3], b1 + (size_t)(c - 14) * 32 * (D_ * 2), l0 + (c & 3) * 8192 + 4096);
      dma4(vd, vd, vd, vd, d1 + (c - 14) * 4096, l0 + (c & 3) * 8192);
    }
  };
  issue(0); issue(1); issue(2);
  f16v acc[4] = {}, aca[4] = {};
  lchar *dA[2], *dB[2], *tA[2], *tB[2];
  const int pp = lane & 3, ps = ((pp & 1) << 1) | (pp >> 1);
#pragma unroll
  for (int b = 0; b < 2; ++b) {
    dA[b] = sm + w * 32768 + ((b ^ (w >> 1)) << 10) + lane * 16;
    dB[b] = sm + w * 32768 + ((b ^ (w & 1)) << 10) + lane * 16;
    tB[b] = sm + w * 32768 + 4096 + (4 * h + q) * 128 + ((b ^ (w & 1) ^ (q >> 1)) << 6) + ((lane >> 4) & 1) * 32 + pp * 8;
    tA[b] = sm + w * 32768 + 4096 + (8 * h + q) * 128 + ((b ^ (w >> 1) ^ (q >> 1)) << 6) + ((lane >> 4) & 1) * 32 + ps * 8;
  }
#pragma unroll
  for (int c = 0; c < 18; ++c) {
    if (c < 16) WAITV(16); else if (c == 16) WAITV(8); else WAITV(0);
    const int sl = (c & 3) * 8192;
    bf8 fa[2][2], fb[2][2];
#pragma unroll
    for (int ks = 0; ks < 2; ++ks)
#pragma unroll
      for (int b = 0; b < 2; ++b) {
        if (c < 14) {
          fb[ks][b] = trd(tB[b] + sl + ks * 2048, tB[b] + sl + ks * 2048 + 1024);
          fa[ks][b] = *LDSP(bf8, dA[b] + sl + ks * 2048);
        } else {
          fa[ks][b] = trd(tA[b] + sl + ks * 2048, tA[b] + sl + ks * 2048 + 512);
          fb[ks][b] = *LDSP(bf8, dB[b] + sl + ks * 2048);
        }
      }
    if (c + 3 < 18) issue(c + 3);
#pragma unroll
    for (int ks = 0; ks < 2; ++ks)
#pragma unroll
      for (int a = 0; a < 4; ++a) {
        if (c < 14) acc[a] = MFMA(fa[ks][a >> 1], fb[ks][a & 1], acc[a]);
        else aca[a] = MFMA(fa[ks][a >> 1], fb[ks][a & 1], aca[a]);
      }
  }
  float g[2], og[2];
#pragma unroll
  for (int b = 0; b < 2; ++b) { g[b] = *LDSP(float, sm + GG + ((b ^ (w & 1)) * 32 + r) * 4); og[b] = rbf(1.f - g[b]); }
  const float bias = og[0] * (float)*LDSP(__bf16, sm + B2 + ((w & 1) * 32 + r) * 2);
#pragma unroll
  for (int a = 0; a < 4; ++a) acc[a] = acc[a] * og[a & 1] + aca[a] * g[a & 1];
  reduce4(acc, sm, w, lane);
  acc[0] += bias;
  char* op = A.out + (size_t)(st * 64 + (w & 1) * 32 + r) * 1024 + (dt * 64 + (w >> 1) * 32 + 8 * h) * 2;
  *(bf8*)op = pack8(acc[0], 0); *(bf8*)(op + 32) = pack8(acc[0], 1);
}

template <int P>
__global__ __launch_bounds__(256, 1) void k_phase(Args A) {
  __shared__ __attribute__((aligned(1024))) char smem[LDS_BYTES];
  lchar* sm = (lchar*)smem;
  if (P == 1) phase1(A, blockIdx.x, sm);
  if (P == 2) phase2(A, blockIdx.x, sm);
  if (P == 3) phase3(A, blockIdx.x, sm);
}

extern "C" void kernel_launch(void* const* d_in, const int* in_sizes, int n_in, void* d_out, int out_size, void* d_ws,
                              size_t ws_size, hipStream_t stream) {
  Args A;
  A.x = (const char*)d_in[0]; A.wqkv = (const char*)d_in[1]; A.wphi = (const char*)d_in[2]; A.wo = (const char*)d_in[3];
  A.w1 = (const char*)d_in[5]; A.b1 = (const char*)d_in[6]; A.w2 = (const char*)d_in[7]; A.b2 = (const char*)d_in[8];
  A.gw = (const char*)d_in[9]; A.out = (char*)d_out; A.ws = (char*)d_ws;
  k_phase<1><<<256, 256, 0, stream>>>(A);
  k_phase<2><<<256, 256, 0, stream>>>(A);
  k_phase<3><<<256, 256, 0, stream>>>(A);
}
```

```cpp
#include <hip/hip_runtime.h>
#include <hip/hip_bf16.h>
#include <stdint.h>

typedef __attribute__((ext_vector_type(8))) __bf16 bf8;
typedef __attribute__((ext_vector_type(4))) __bf16 bf4;
typedef __attribute__((ext_vector_type(2))) __bf16 bf2;
typedef __attribute__((ext_vector_type(2))) float f2;
typedef __attribute__((ext_vector_type(4))) float f4;
typedef __attribute__((ext_vector_type(8))) float f8;
typedef __attribute__((ext_vector_type(16))) float f16v;
typedef __attribute__((ext_vector_type(4))) unsigned u4;
typedef __attribute__((ext_vector_type(2))) unsigned u2;
typedef __attribute__((address_space(3))) char lchar;
#define LDSP(T, a) ((__attribute__((address_space(3))) T*)(a))
#define DI __device__ __forceinline__

constexpr int S_ = 2048, D_ = 512, F_ = 1792;
constexpr size_t WS_H = 0, WS_PQ = 2u << 20, WS_KV = 6u << 20, WS_G = 8u << 20, WS_U = 9u << 20, WS_O = 11u << 20;
constexpr int KVP = 544;
constexpr int LDS_BYTES = 163328;
constexpr size_t WS_FL = 13u << 20;
constexpr int FL_H = 64, FL_R = 320, FL_K = 576, FL_U = 832, FL_O = 1344;
typedef unsigned long long u64;

struct Args {
  const char *x, *wqkv, *wphi, *wo, *w1, *b1, *w2, *b2, *gw;
  char *out, *ws;
};

#define STAMP(k)
#define STAMP4(k)
#define TK(k)
#define TS()
#define HC 1
#define PR 3
#define PF0 0
#define R2 0
#define HP 1
#define NOPF2
#define PC 4
#define US 8
#define OS 40
#define MFMA(a, b, c) __builtin_amdgcn_mfma_f32_32x32x16_bf16(a, b, c, 0, 0, 0)
#define WAITV(n) asm volatile("s_waitcnt vmcnt(%0) lgkmcnt(0)" ::"i"(n) : "memory")
#define BAR() do { asm volatile("s_waitcnt lgkmcnt(0)" ::: "memory"); __builtin_amdgcn_s_barrier(); asm volatile("" ::: "memory"); } while (0)

DI void dma4(unsigned v0, unsigned v1, unsigned v2, unsigned v3, const char* base, unsigned m0) {
  asm volatile("s_mov_b32 m0, %5\n\ts_nop 0\n\tglobal_load_lds_dwordx4 %0, %4\n\tglobal_load_lds_dwordx4 %1, %4 offset:1024\n\t"
               "global_load_lds_dwordx4 %2, %4 offset:2048\n\tglobal_load_lds_dwordx4 %3, %4 offset:3072"
               ::"v"(v0), "v"(v1), "v"(v2), "v"(v3), "s"(base), "s"(m0) : "memory");
}
DI void dma4c(unsigned v0, const char* base, unsigned m0) {
  asm volatile("s_mov_b32 m0, %2\n\ts_nop 0\n\tglobal_load_lds_dwordx4 %0, %1 sc1\n\tglobal_load_lds_dwordx4 %0, %1 offset:1024 sc1\n\t"
               "global_load_lds_dwordx4 %0, %1 offset:2048 sc1\n\tglobal_load_lds_dwordx4 %0, %1 offset:3072 sc1"
               ::"v"(v0), "s"(base), "s"(m0) : "memory");
}
DI void dma3(unsigned v0, const char* base, unsigned m0) {
  asm volatile("s_mov_b32 m0, %2\n\ts_nop 0\n\tglobal_load_lds_dwordx4 %0, %1\n\tglobal_load_lds_dwordx4 %0, %1 offset:1024\n\t"
               "global_load_lds_dwordx4 %0, %1 offset:2048" ::"v"(v0), "s"(base), "s"(m0) : "memory");
}
DI void dma2(unsigned v0, const char* base, unsigned m0) {
  asm volatile("s_mov_b32 m0, %2\n\ts_nop 0\n\tglobal_load_lds_dwordx4 %0, %1\n\tglobal_load_lds_dwordx4 %0, %1 offset:1024"
               ::"v"(v0), "s"(base), "s"(m0) : "memory");
}
DI void dma1(unsigned v0, const char* base, unsigned m0) {
  asm volatile("s_mov_b32 m0, %2\n\ts_nop 0\n\tglobal_load_lds_dwordx4 %0, %1" ::"v"(v0), "s"(base), "s"(m0) : "memory");
}
DI void dma1w(unsigned v0, const char* base, unsigned m0) {
  asm volatile("s_mov_b32 m0, %2\n\ts_nop 0\n\tglobal_load_lds_dword %0, %1 sc1" ::"v"(v0), "s"(base), "s"(m0) : "memory");
}
DI void pf2(unsigned v0, unsigned v1, const char* base, unsigned m0) {
  asm volatile("s_mov_b32 m0, %3\n\ts_nop 0\n\tglobal_load_lds_dword %0, %2\n\tglobal_load_lds_dword %1, %2" ::"v"(v0), "v"(v1), "s"(base), "s"(m0) : "memory");
}
DI void pf2(unsigned v0, unsigned v1, const char* base, unsigned m0, const char* base1) {
  asm volatile("s_mov_b32 m0, %3\n\ts_nop 0\n\tglobal_load_lds_dword %0, %2\n\tglobal_load_lds_dword %1, %4" ::"v"(v0), "v"(v1), "s"(base), "s"(m0), "s"(base1) : "memory");
}
DI bf8 trd(lchar* p0, lchar* p1) {
  bf4 a = __builtin_amdgcn_ds_read_tr16_b64_v4bf16(LDSP(bf4, p0));
  bf4 b = __builtin_amdgcn_ds_read_tr16_b64_v4bf16(LDSP(bf4, p1));
  return __builtin_shufflevector(a, b, 0, 1, 2, 3, 4, 5, 6, 7);
}
DI bf8 pack8(const f16v& c, int s) {
  f8 t = s ? __builtin_shufflevector(c, c, 8, 9, 10, 11, 12, 13, 14, 15) : __builtin_shufflevector(c, c, 0, 1, 2, 3, 4, 5, 6, 7);
  return __builtin_convertvector(t, bf8);
}
DI float rbf(float x) { return (float)(__bf16)x; }
DI float bflo(unsigned u) { return __builtin_bit_cast(float, u << 16); }
DI float bfhi(unsigned u) { return __builtin_bit_cast(float, u & 0xffff0000u); }
DI float xhalf_max(float a) { auto r = __builtin_amdgcn_permlane32_swap(__builtin_bit_cast(unsigned, a), __builtin_bit_cast(unsigned, a), false, false);
  unsigned r0 = r[0], r1 = r[1]; return fmaxf(__builtin_bit_cast(float, r0), __builtin_bit_cast(float, r1)); }
DI float xhalf_add(float a) { auto r = __builtin_amdgcn_permlane32_swap(__builtin_bit_cast(unsigned, a), __builtin_bit_cast(unsigned, a), false, false);
  unsigned r0 = r[0], r1 = r[1]; return __builtin_bit_cast(float, r0) + __builtin_bit_cast(float, r1); }

template <int C> DI float dppf(float v) { return __builtin_bit_cast(float, __builtin_amdgcn_update_dpp(0, __builtin_bit_cast(int, v), C, 0xf, 0xf, true)); }
DI float wave_sum(float v) {
  v += dppf<0xB1>(v); v += dppf<0x4E>(v); v += dppf<0x141>(v); v += dppf<0x140>(v);
  auto r = __builtin_amdgcn_permlane16_swap(__builtin_bit_cast(unsigned, v), __builtin_bit_cast(unsigned, v), false, false);
  unsigned r0 = r[0], r1 = r[1];
  return xhalf_add(__builtin_bit_cast(float, r0) + __builtin_bit_cast(float, r1));
}
DI void dual_softmax(const f16v& c, int s, bf8& pos, bf8& neg) {
  const float L2E = 1.4426950408889634f;
  float mx = c[8 * s], mn = c[8 * s];
#pragma unroll
  for (int j = 1; j < 8; ++j) { mx = fmaxf(mx, c[8 * s + j]); mn = fminf(mn, c[8 * s + j]); }
  mx = xhalf_max(mx) * -L2E; mn = xhalf_max(-mn) * -L2E;
  f8 ep, en;
#pragma unroll
  for (int j = 0; j < 8; ++j) { ep[j] = __builtin_amdgcn_exp2f(__builtin_fmaf(c[8 * s + j], L2E, mx)); en[j] = __builtin_amdgcn_exp2f(__builtin_fmaf(c[8 * s + j], -L2E, mn)); }
  float sp = ((ep[0] + ep[1]) + (ep[2] + ep[3])) + ((ep[4] + ep[5]) + (ep[6] + ep[7]));
  float sn = ((en[0] + en[1]) + (en[2] + en[3])) + ((en[4] + en[5]) + (en[6] + en[7]));
  sp = __builtin_amdgcn_rcpf(xhalf_add(sp)); sn = __builtin_amdgcn_rcpf(xhalf_add(sn));
  pos = __builtin_convertvector(ep * sp, bf8); neg = __builtin_convertvector(en * sn, bf8);
}
typedef __amdgpu_buffer_rsrc_t rsrc_t;
DI void st16(rsrc_t rs, unsigned off, u4 v) { __builtin_amdgcn_raw_buffer_store_b128(v, rs, off, 0, 16); }
DI void st16(rsrc_t rs, unsigned off, bf8 v) { st16(rs, off, __builtin_bit_cast(u4, v)); }
DI u4 ld16(rsrc_t rs, unsigned off) { return __builtin_amdgcn_raw_buffer_load_b128(rs, off, 0, 16); }
DI u64 ld_flag(const Args& A, int i) { return __hip_atomic_load((const u64*)(A.ws + WS_FL) + i, __ATOMIC_RELAXED, __HIP_MEMORY_SCOPE_AGENT); }
DI void st_flag(const Args& A, int i, u64 v) { __hip_atomic_store((u64*)(A.ws + WS_FL) + i, v, __ATOMIC_RELAXED, __HIP_MEMORY_SCOPE_AGENT); }
template <int SL> DI void wait_flag(const Args& A, int i, u64 ep) {
  while (!__all(ld_flag(A, i) == ep)) __builtin_amdgcn_s_sleep(SL);
  asm volatile("" ::: "memory");
}
DI void arrive(const Args& A, lchar* cnt, unsigned n, int i, u64 ep, int lane) {
  if (lane == 0 && __hip_atomic_fetch_add(LDSP(unsigned, cnt), 1u, __ATOMIC_RELAXED, __HIP_MEMORY_SCOPE_WORKGROUP) == n - 1) st_flag(A, i, ep);
}
struct TrOff { unsigned v[4]; };
DI TrOff tr_off(int lane, int pitch) {
  TrOff o;
#pragma unroll
  for (int j = 0; j < 4; ++j) o.v[j] = (8 * j + (lane >> 3)) * pitch + (((lane & 7) ^ ((lane >> 2) & 4)) << 4) - j * 1024;
  return o;
}
DI void red_put(const f16v (&acc)[4], lchar* p) {
#pragma unroll
  for (int i = 0; i < 4; ++i)
#pragma unroll
    for (int q = (i ? 0 : 2); q < 4; ++q) {
      f4 t = {acc[i][4 * q], acc[i][4 * q + 1], acc[i][4 * q + 2], acc[i][4 * q + 3]};
      *LDSP(f4, p + (i * 4 + q) * 1024) = t;
    }
}
DI f8 red_get(f8 v, lchar* sm, int tg, int role, int lane) {
#pragma unroll
  for (int i = 0; i < 4; ++i) {
    if (i == 0 && role == 0) continue;
    lchar* p = sm + (tg ^ i) * 32768 + 16384 + (i * 4 + 2 * role) * 1024 + lane * 16;
    f4 a = *LDSP(f4, p), b = *LDSP(f4, p + 1024);
    v += __builtin_shufflevector(a, b, 0, 1, 2, 3, 4, 5, 6, 7);
  }
  return v;
}

template <int P>
__global__ __launch_bounds__(512, 2) void k_main(Args A) {
  __shared__ __attribute__((aligned(1024))) char smem[LDS_BYTES];
  lchar* sm = (lchar*)smem;
  constexpr bool FU = P == 0;
  const int tid = threadIdx.x, lane = tid & 63, w = __builtin_amdgcn_readfirstlane(tid >> 6), tg = w & 3, role = w >> 2;
  const int bid = blockIdx.x, xcd = bid & 7, bi = bid >> 3;
  const unsigned l0 = (unsigned)(size_t)sm;
  const int r = lane & 31, h = lane >> 5, q4 = (lane & 15) >> 2, g16 = (lane >> 4) & 1, pp = lane & 3, ps = ((pp & 1) << 1) | (pp >> 1);
  const rsrc_t rs = __builtin_amdgcn_make_buffer_rsrc(A.ws, 0, 16 << 20, 0x00020000);
  const unsigned vd = lane * 16;
  const unsigned lw = l0 + tg * 32768;
  constexpr int SCR = 131072, BIAS = 153600, GG = 153856, B2 = 154112, EPO = 154368, DUM = 154624, QX = 154880, CNT = 163072;
  const int dt = (xcd & 1) * 4 + (bi & 3), ft = (xcd >> 1) * 7 + (bi >> 2), st = (xcd >> 1) * 8 + (bi >> 2);
  const bool owg = bi >= 28;
  u64 ep = 0;
  bool hrdy = false;
  bf8 qp[2], qn[2];
  const int tt = (xcd >> 1) * 4 + (bi >> 3), ct = (xcd & 1) * 8 + (bi & 7);
  const int rot2 = R2 == 0 ? 4 * (bi & 3) + 2 * (bi >> 2) + (xcd & 1) : R2 == 1 ? 8 * ((bi + (bi >> 2)) & 1) : R2 == 2 ? 4 * ((bi + (bi >> 2)) & 3) : 0;
  const char* db2 = A.ws + WS_H + (size_t)(dt * 128 + tg * 32) * 2048;
  const char* tb2 = A.w1 + (size_t)(tg * 512) * (F_ * 2) + ft * 128;
  const TrOff to2 = tr_off(lane, F_ * 2);
  auto iss2T = [&](int c) { dma4(to2.v[0], to2.v[1], to2.v[2], to2.v[3], tb2 + (size_t)((c + rot2) & 15) * 32 * (F_ * 2), lw + (c & 3) * 8192 + 4096); };
  auto iss2D = [&](int c) { dma4c(vd, db2 + ((c + rot2) & 15) * 4096, lw + (c & 3) * 8192); };

  if (P <= 1) {
    STAMP(0);
    constexpr int SLOT = 28672, LNX = 143360, GW = 151552, WP = 152576;
    const int tok0 = tt * 128 + ct * 8, rot = (bi & 7) + 2 * (bi >> 3);
    lchar* xr = sm + tg * 4096 + r * 128;
    const int xs = (r >> 1) & 7;
    lchar* wr = sm + 16384 + (8 * h + q4) * 64 + g16 * 32 + pp * 8;
    if (role == 0) {
      unsigned vx[4];
#pragma unroll
      for (int j = 0; j < 4; ++j) { int row = 8 * j + (lane >> 3); vx[j] = row * 1024 + (((lane & 7) ^ ((row >> 1) & 7)) << 4) - j * 1024; }
      const unsigned vw = (lane >> 2) * 3072 + (lane & 3) * 16;
      const char* xb = A.x + (size_t)(tt * 128 + 32 * tg) * 1024;
      const char* wb = A.wqkv + (size_t)(16 * tg) * 3072 + ct * 64;
      auto issue = [&](int c) {
        const unsigned sl = l0 + (c % 5) * SLOT; const int cc = (c + rot) & 7;
        dma4(vx[0], vx[1], vx[2], vx[3], xb + cc * 128, sl + tg * 4096);
        dma3(vw, wb + (size_t)cc * 64 * 3072, sl + 16384 + tg * 3072);
      };
      BAR();
#pragma unroll
      for (int c = 0; c < 4; ++c) issue(c);
      f16v aq = {};
#pragma unroll
      for (int c = 0; c < 8; ++c) {
        if (c <= 4) WAITV(21); else if (c == 5) WAITV(14); else if (c == 6) WAITV(7); else WAITV(0);
        BAR();
        const int sl = (c % 5) * SLOT;
        bf8 xf[4], wq[4];
#pragma unroll
        for (int ks = 0; ks < 4; ++ks) {
          lchar* p = wr + sl + ks * 3072;
          xf[ks] = *LDSP(bf8, xr + sl + (((2 * ks + h) ^ xs) << 4));
          wq[ks] = trd(p, p + 256);
        }
        if (FU && c == 0) ep = *LDSP(u64, sm + EPO) + 1;
#pragma unroll
        for (int ks = 0; ks < 4; ++ks) aq = MFMA(wq[ks], xf[ks], aq);
        if (c + 4 < 8) issue(c + 4);
      }
      *LDSP(bf8, sm + QX + tg * 2048 + vd) = pack8(aq, 0); *LDSP(bf8, sm + QX + tg * 2048 + 1024 + vd) = pack8(aq, 1);
      BAR();
      STAMP(2);
      if (FU && !owg) {
        hrdy = HP && __all(ld_flag(A, FL_H + tg * 64 + lane) == ep);
        asm volatile("" ::: "memory");
        dma1w((lane & 31) * 4, A.b1 + ft * 128, l0 + BIAS); iss2T(0); iss2T(1); iss2T(2);
        if (hrdy) { iss2D(0); iss2D(1); iss2D(2); }
      }
    } else {
      const int t2 = tid - 256;
      __builtin_amdgcn_s_setprio(PR);
      u64 tk[6];
      dma2(vd, A.x + (size_t)(tok0 + 2 * tg) * 1024, l0 + LNX + tg * 2048);
      dma1(vd, A.gw, l0 + GW);
      if (FU) dma1w((lane & 1) * 4, A.ws + WS_FL, l0 + EPO);
      dma1(vd, A.wphi + ct * 1024, l0 + WP);
      *LDSP(unsigned, sm + CNT + (lane & 7) * 4) = 0;
      if (PF0) {
        const int i = bi * 4 + tg;
        pf2((lane & 31) * 128, (lane >> 4) * 3072 + min((lane & 15) >> 2, 2) * 1024 + (lane & 3) * 128, A.x + (size_t)((xcd >> 1) * 512 + i * 4) * 1024, l0 + DUM,
            A.wqkv + (size_t)i * 4 * 3072 + (xcd & 1) * 512);
      }
      BAR();
      WAITV(PF0 ? 2 : 0);
      TK(0);
      const u4 gwv = *LDSP(u4, sm + GW + vd);
#pragma unroll
      for (int i = 0; i < 2; ++i) {
        lchar* p = sm + LNX + (2 * tg + i) * 1024 + vd;
        u4 xv = *LDSP(u4, p);
        float v[8], s = 0.f, gs = 0.f;
#pragma unroll
        for (int j = 0; j < 4; ++j) { v[2 * j] = bflo(xv[j]); v[2 * j + 1] = bfhi(xv[j]);
          s += v[2 * j] + v[2 * j + 1]; gs += rbf(v[2 * j] * bflo(gwv[j])) + rbf(v[2 * j + 1] * bfhi(gwv[j])); }
        s = wave_sum(s) * (1.f / 512.f); gs = wave_sum(gs);
        float vs = 0.f;
#pragma unroll
        for (int j = 0; j < 8; ++j) { float d = v[j] - s; vs += d * d; }
        vs = wave_sum(vs) * (1.f / 512.f);
        const float m = rbf(s), isd = __builtin_amdgcn_rcpf(rbf(__builtin_amdgcn_sqrtf(rbf(rbf(vs) + 1e-5f))));
        f8 o;
#pragma unroll
        for (int j = 0; j < 8; ++j) o[j] = rbf(v[j] - m) * isd;
        *LDSP(bf8, p) = __builtin_convertvector(o, bf8);
        const float g = rbf(__builtin_amdgcn_rcpf(1.f + __builtin_amdgcn_exp2f(rbf(gs) * -1.4426950408889634f)));
        *LDSP(float, sm + GG + (2 * tg + i) * 4) = g;
      }
      ep = *LDSP(u64, sm + EPO) + 1;
      asm volatile("s_waitcnt lgkmcnt(0)" ::: "memory");
      if (lane == 0) __hip_atomic_fetch_add(LDSP(unsigned, sm + CNT + 20), 1u, __ATOMIC_RELAXED, __HIP_MEMORY_SCOPE_WORKGROUP);
      while (__hip_atomic_load(LDSP(unsigned, sm + CNT + 20), __ATOMIC_RELAXED, __HIP_MEMORY_SCOPE_WORKGROUP) != 4u) __builtin_amdgcn_s_sleep(1);
      asm volatile("" ::: "memory");
      {
          unsigned t[8];
#pragma unroll
          for (int j = 0; j < 8; ++j) t[j] = *LDSP(unsigned, sm + LNX + j * 1024 + t2 * 4);
          u4 lo, hi;
#pragma unroll
          for (int j = 0; j < 4; ++j) { lo[j] = (t[2 * j] & 0xffffu) | (t[2 * j + 1] << 16); hi[j] = (t[2 * j] >> 16) | (t[2 * j + 1] & 0xffff0000u); }
          const int d = 2 * t2, dl = d & 31, rr = (dl & 19) | ((dl & 4) << 1) | ((dl & 8) >> 1);
          const unsigned hp = WS_H + (((d >> 6) * 128 + (tok0 >> 4)) * 2 + ((d >> 5) & 1)) * 1024 + (((tok0 >> 3) & 1) * 32 + rr) * 16;
          st16(rs, hp, lo); st16(rs, hp + 16, hi);
          if (t2 < 2) st16(rs, WS_G + tok0 * 4 + t2 * 16, *LDSP(u4, sm + GG + t2 * 16));
        }
      TK(1);
      f16v ak = {}, av = {};
#pragma unroll
      for (int c = 0; c < 8; ++c) {
        BAR();
        if (c == 0) TK(2); if (c == 3) TK(4); if (c == 5) TK(5);
        if (c == PC) { const unsigned o = (lane >> 3) * (F_ * 2) + min(lane & 7, 6) * 128;
          pf2(o, o + 8 * F_ * 2, A.w1 + (size_t)((bi * 4 + tg) * 16) * (F_ * 2) + (xcd >> 1) * 896, l0 + DUM); }
        const int sl = (c % 5) * SLOT;
        bf8 xf[4], wk[4], wv[4];
#pragma unroll
        for (int ks = 0; ks < 4; ++ks) {
          lchar* p = wr + sl + ks * 3072;
          xf[ks] = *LDSP(bf8, xr + sl + (((2 * ks + h) ^ xs) << 4));
          wk[ks] = trd(p + 1024, p + 1280); wv[ks] = trd(p + 2048, p + 2304);
        }
#pragma unroll
        for (int ks = 0; ks < 4; ++ks) { ak = MFMA(wk[ks], xf[ks], ak); av = MFMA(xf[ks], wv[ks], av); }
        if (c == HC) { WAITV(PC <= HC ? 2 : 0); arrive(A, sm + CNT + 24, 4, FL_H + tt * 16 + ct, ep, lane); TK(3); }
      }
      BAR();
      lchar* wpp = sm + WP + g16 * 512 + (4 * h + q4) * 32 + pp * 8;
      const f16v z16 = {};
      const bf8 z8 = {};
      const bf8 wf = trd(wpp, wpp + 256), wa = g16 ? z8 : wf, wb = g16 ? wf : z8;
      f16v pk = MFMA(wa, pack8(ak, 0), z16); pk = MFMA(wb, pack8(ak, 1), pk);
      f16v pq = MFMA(wa, *LDSP(bf8, sm + QX + tg * 2048 + vd), z16); pq = MFMA(wb, *LDSP(bf8, sm + QX + tg * 2048 + 1024 + vd), pq);
      lchar* im = sm + SCR + tg * (2 * KVP * 4);
#pragma unroll
      for (int s = 0; s < 2; ++s) {
        bf8 kp, kn; dual_softmax(pk, s, kp, kn);
        *LDSP(bf8, im + s * 2048 + r * 64 + h * 16) = kp; *LDSP(bf8, im + s * 2048 + r * 64 + 32 + h * 16) = kn;
      }
      const bf8 v0 = pack8(av, 0), v1 = pack8(av, 1);
      const __bf16 o1 = (__bf16)1.f; const bf8 one = {o1, o1, o1, o1, o1, o1, o1, o1};
      lchar* tp = im + (4 * h + q4) * 64 + g16 * 32 + ps * 8;
      f16v kv[2];
#pragma unroll
      for (int s = 0; s < 2; ++s) {
        bf8 a0 = trd(tp + s * 2048, tp + s * 2048 + 512), a1 = trd(tp + s * 2048 + 1024, tp + s * 2048 + 1536);
        kv[s] = MFMA(a0, g16 == s ? v0 : one, z16); kv[s] = MFMA(a1, g16 == s ? v1 : one, kv[s]);
      }
      asm volatile("s_waitcnt lgkmcnt(0)" ::: "memory");
#pragma unroll
      for (int s = 0; s < 2; ++s) {
        const bool own = g16 == s;
        lchar* p = im + s * (KVP * 4) + (own ? (h * 16 + (r & 15)) * 32 : 2048 + h * 32);
        const int st2 = own ? 1024 : 64;
        if (own || (r & 15) == 0) {
          *LDSP(f4, p) = __builtin_shufflevector(kv[s], kv[s], 0, 1, 2, 3); *LDSP(f4, p + 16) = __builtin_shufflevector(kv[s], kv[s], 4, 5, 6, 7);
          *LDSP(f4, p + st2) = __builtin_shufflevector(kv[s], kv[s], 8, 9, 10, 11); *LDSP(f4, p + st2 + 16) = __builtin_shufflevector(kv[s], kv[s], 12, 13, 14, 15);
        }
      }
      asm volatile("s_waitcnt lgkmcnt(0)" ::: "memory");
      if (lane == 0) __hip_atomic_fetch_add(LDSP(unsigned, sm + CNT), 1u, __ATOMIC_RELAXED, __HIP_MEMORY_SCOPE_WORKGROUP);
      while (__hip_atomic_load(LDSP(unsigned, sm + CNT), __ATOMIC_RELAXED, __HIP_MEMORY_SCOPE_WORKGROUP) != 4u) __builtin_amdgcn_s_sleep(1);
      asm volatile("" ::: "memory");
      for (int t = t2; t < 2 * (KVP / 4); t += 256) {
        f4 s = *LDSP(f4, sm + SCR + t * 16);
#pragma unroll
        for (int k = 1; k < 4; ++k) s += *LDSP(f4, sm + SCR + k * 2 * KVP * 4 + t * 16);
        const int hd = t >= KVP / 4, e = t - hd * (KVP / 4);
        st16(rs, WS_KV + ((2 * ct + hd) * 16 + tt) * (KVP * 4) + e * 16, __builtin_bit_cast(u4, s));
      }
      dual_softmax(pq, 0, qp[0], qn[0]); dual_softmax(pq, 1, qp[1], qn[1]);
      asm volatile("s_waitcnt vmcnt(0)" ::: "memory");
      arrive(A, sm + CNT + 4, 4, FL_K + ct * 16 + tt, ep, lane);
      if (w == 4) { STAMP4(3); TS(); }
    }
  }

  const int rot3 = (7 * (bi & 3) / 2 + 2 * (bi >> 2) + (xcd & 1)) % 14, rot1 = (bi + (bi >> 2)) & 3;
  const char* d2 = A.ws + WS_U + (size_t)(dt * 112 + tg * 28) * 2048;
  const char* b2 = A.w2 + (size_t)(tg * 448) * (S_ * 2) + st * 128;
  const char* d1 = A.ws + WS_O + (size_t)(st * 32 + tg * 8) * 2048;
  const char* b1 = A.wo + (size_t)(tg * 128) * (D_ * 2) + dt * 128;
  auto iss3T = [&](int c) {
    if (c >= 14) { const TrOff t = tr_off(lane, D_ * 2); dma4(t.v[0], t.v[1], t.v[2], t.v[3], b1 + (size_t)((c + rot1) & 3) * 32 * (D_ * 2), lw + (c & 3) * 8192); }
    else { const TrOff t = tr_off(lane, S_ * 2); int cc = c + rot3; if (cc >= 14) cc -= 14;
           dma4(t.v[0], t.v[1], t.v[2], t.v[3], b2 + (size_t)cc * 32 * (S_ * 2), lw + (c & 3) * 8192 + 4096); }
  };
  auto iss3D = [&](int c) {
    if (c >= 14) dma4c(vd, d1 + ((c + rot1) & 3) * 4096, lw + (c & 3) * 8192);
    else { int cc = c + rot3; if (cc >= 14) cc -= 14; dma4c(vd, d2 + cc * 4096, lw + (c & 3) * 8192); }
  };

  if (FU || P == 2) {
    if (owg) {
      if (role == 0) {
        const int head = xcd * 4 + bi - 28;
        __builtin_amdgcn_s_sleep(100);
        if (tg < 3) {
        wait_flag<16>(A, FL_K + (head >> 1) * 16 + (lane & 15), ep);
        STAMP(5);
        if (tid < KVP / 4) {
          u4 t[16];
#pragma unroll
          for (int k = 0; k < 16; ++k) t[k] = ld16(rs, WS_KV + (head * 16 + k) * (KVP * 4) + tid * 16);
          f4 s = {};
#pragma unroll
          for (int k = 0; k < 16; ++k) s += __builtin_bit_cast(f4, t[k]);
          __builtin_amdgcn_raw_buffer_store_b64(__builtin_bit_cast(u2, __builtin_convertvector(s, bf4)), rs, WS_PQ + head * 2048 + tid * 8, 0, 16);
        }
        asm volatile("s_waitcnt vmcnt(0)" ::: "memory");
        arrive(A, sm + CNT + 16, 3, FL_R + (head >> 1) * 16 + (head & 1), ep, lane);
        }
        STAMP(6);
        iss3T(0); iss3T(1); iss3T(2);
      }
    } else {
      f8 v = {};
      if (role == 0) {
        if (!FU) { dma1w((lane & 31) * 4, A.b1 + ft * 128, l0 + BIAS); iss2T(0); iss2T(1); iss2T(2); }
        if (!hrdy) { wait_flag<4>(A, FL_H + tg * 64 + lane, ep); iss2D(0); iss2D(1); iss2D(2); }
        f16v acc[4] = {};
        lchar *dr[2], *tr[2];
#pragma unroll
        for (int b = 0; b < 2; ++b) {
          dr[b] = sm + tg * 32768 + ((b ^ (tg & 1)) << 10) + vd;
          tr[b] = sm + tg * 32768 + 4096 + (8 * h + q4) * 128 + ((b ^ (tg >> 1) ^ (q4 >> 1)) << 6) + g16 * 32 + pp * 8;
        }
#pragma unroll
        for (int c = 0; c < 16; ++c) {
          if (c == 0 || c == 14) WAITV(8); else if (c == 1) WAITV(12); else if (c < 14) WAITV(16); else if (FU) WAITV(4); else WAITV(0);
          if (c == 0) STAMP(5);
          const int sl = (c & 3) * 8192;
          bf8 d[2][2], t[2][2];
#pragma unroll
          for (int ks = 0; ks < 2; ++ks)
#pragma unroll
            for (int b = 0; b < 2; ++b) {
              t[ks][b] = trd(tr[b] + sl + ks * 2048, tr[b] + sl + ks * 2048 + 512);
              d[ks][b] = *LDSP(bf8, dr[b] + sl + ks * 2048);
            }
          if (c + 3 < 16) { iss2T(c + 3); iss2D(c + 3); }
          if (FU && c == 14) iss3T(0);
          if (FU && c == 15) iss3T(1);
#pragma unroll
          for (int ks = 0; ks < 2; ++ks)
#pragma unroll
            for (int a = 0; a < 4; ++a) acc[a] = MFMA(t[ks][a >> 1], d[ks][a & 1], acc[a]);
        }
        STAMP(6);
        asm volatile("s_waitcnt lgkmcnt(0)" ::: "memory");
        red_put(acc, sm + tg * 32768 + 16384 + vd);
        v = __builtin_shufflevector(acc[0], acc[0], 0, 1, 2, 3, 4, 5, 6, 7);
      }
      BAR();
      v = red_get(v, sm, tg, role, lane);
      const int fb = tg >> 1, dbk = tg & 1;
      f8 u;
#pragma unroll
      for (int g = 0; g < 2; ++g) {
        u2 bb = *LDSP(u2, sm + BIAS + (fb * 32 + 16 * role + 8 * g + 4 * h) * 2);
        float bv[4] = {bflo(bb[0]), bfhi(bb[0]), bflo(bb[1]), bfhi(bb[1])};
#pragma unroll
        for (int j = 0; j < 4; ++j) {
          const float x = v[4 * g + j] + bv[j];
          const float y = x * __builtin_fmaf(x * x, -0.1029432397f, -2.3022081985f);
          u[4 * g + j] = x * __builtin_amdgcn_rcpf(1.f + __builtin_amdgcn_exp2f(y));
        }
      }
      st16(rs, WS_U + ((dt * 112 + ft * 4 + fb * 2 + role) * 2 + dbk) * 1024 + vd, __builtin_convertvector(u, bf8));
      if (FU) {
        BAR();
        if (role == 0) { iss3T(2); WAITV(4); } else WAITV(0);
        arrive(A, sm + CNT + 8, 8, FL_U + (dt * 4 + (xcd >> 1)) * 16 + (bi >> 2), ep, lane);
      }
    }
    STAMP(7);
  }

  if (FU || P == 3) {
    f8 v = {};
    if (role == 0) {
      f16v acc[4] = {}, aca[4] = {};
      lchar *dA[2], *dB[2], *tA[2], *tB[2];
#pragma unroll
      for (int b = 0; b < 2; ++b) {
        dA[b] = sm + tg * 32768 + ((b ^ (tg >> 1)) << 10) + vd;
        dB[b] = sm + tg * 32768 + ((b ^ (tg & 1)) << 10) + vd;
        tB[b] = sm + tg * 32768 + 4096 + (4 * h + q4) * 128 + ((b ^ (tg & 1) ^ (q4 >> 1)) << 6) + g16 * 32 + pp * 8;
        tA[b] = sm + tg * 32768 + (8 * h + q4) * 128 + ((b ^ (tg >> 1) ^ (q4 >> 1)) << 6) + g16 * 32 + ps * 8;
      }
      iss3T(14); iss3T(15); iss3T(16); iss3T(17);
      WAITV(0);
      bf8 wo_[4][2][2];
#pragma unroll
      for (int c = 14; c < 18; ++c)
#pragma unroll
        for (int ks = 0; ks < 2; ++ks)
#pragma unroll
          for (int b = 0; b < 2; ++b) wo_[c - 14][ks][b] = trd(tA[b] + (c & 3) * 8192 + ks * 2048, tA[b] + (c & 3) * 8192 + ks * 2048 + 512);
      asm volatile("s_waitcnt lgkmcnt(0)" ::: "memory");
      wait_flag<US>(A, FL_U + (dt * 4 + tg) * 16 + min(lane, 6), ep);
      STAMP(9);
      dma1w(lane * 4, A.ws + WS_G + st * 256, l0 + GG); dma1w((lane & 31) * 4, A.b2 + st * 128, l0 + B2);
      iss3D(0); iss3D(1); iss3D(2);
#pragma unroll
      for (int c = 0; c < 18; ++c) {
        if (c == 0 || (c >= 13 && c <= 15)) WAITV(8); else if (c == 1 || c == 12) WAITV(12); else if (c < 12) WAITV(16); else if (c == 16) WAITV(4); else WAITV(0);
        if (c == 11) {
          while (__hip_atomic_load(LDSP(unsigned, sm + CNT + 28), __ATOMIC_RELAXED, __HIP_MEMORY_SCOPE_WORKGROUP) == 0u) __builtin_amdgcn_s_sleep(1);
          asm volatile("" ::: "memory");
          STAMP(4);
        }
        const int sl = (c & 3) * 8192;
        bf8 fa[2][2], fb[2][2];
#pragma unroll
        for (int ks = 0; ks < 2; ++ks)
#pragma unroll
          for (int b = 0; b < 2; ++b) {
            if (c < 14) {
              fb[ks][b] = trd(tB[b] + sl + ks * 2048, tB[b] + sl + ks * 2048 + 1024);
              fa[ks][b] = *LDSP(bf8, dA[b] + sl + ks * 2048);
            } else {
              fa[ks][b] = wo_[c < 14 ? 0 : c - 14][ks][b];
              fb[ks][b] = *LDSP(bf8, dB[b] + sl + ks * 2048);
            }
          }
        if (c + 3 < 14) iss3T(c + 3);
        if (c + 3 < 18) iss3D(c + 3);
#pragma unroll
        for (int ks = 0; ks < 2; ++ks)
#pragma unroll
          for (int a = 0; a < 4; ++a) {
            if (c < 14) acc[a] = MFMA(fa[ks][a >> 1], fb[ks][a & 1], acc[a]);
            else aca[a] = MFMA(fa[ks][a >> 1], fb[ks][a & 1], aca[a]);
          }
      }
      STAMP(10);
      float g[2];
#pragma unroll
      for (int b = 0; b < 2; ++b) g[b] = *LDSP(float, sm + GG + ((b ^ (tg & 1)) * 32 + r) * 4);
#pragma unroll
      for (int a = 0; a < 4; ++a) acc[a] = acc[a] * rbf(1.f - g[a & 1]) + aca[a] * g[a & 1];
      red_put(acc, sm + tg * 32768 + 16384 + vd);
      v = __builtin_shufflevector(acc[0], acc[0], 0, 1, 2, 3, 4, 5, 6, 7);
    } else {
      wait_flag<12>(A, FL_R + ct * 16 + (lane & 1), ep);
      const f16v z16 = {};
      const int dd = (r & 3) | ((r & 4) << 1) | ((r & 8) >> 1), sb = tt * 4 + tg;
      const unsigned ia = WS_PQ + ct * 4096 + (r < 16 ? (h * 16 + dd) * 16 : 1024 + h * 16), ib = ia + (r < 16 ? 512 : 32);
      u4 im4[4] = {ld16(rs, ia), ld16(rs, ib), ld16(rs, ia + 2048), ld16(rs, ib + 2048)};
#pragma unroll
      for (int s = 0; s < 2; ++s) {
        f16v c = MFMA(__builtin_bit_cast(bf8, im4[2 * s]), qp[s], z16); c = MFMA(__builtin_bit_cast(bf8, im4[2 * s + 1]), qn[s], c);
        const float inv = __builtin_amdgcn_rcpf(c[8] + 1e-6f);
        f8 o = __builtin_shufflevector(c, c, 0, 1, 2, 3, 4, 5, 6, 7) * inv;
        st16(rs, WS_O + (((sb >> 1) * 32 + 2 * ct + s) * 2 + (sb & 1)) * 1024 + vd, __builtin_convertvector(o, bf8));
      }
      asm volatile("s_waitcnt vmcnt(0)" ::: "memory");
      arrive(A, sm + CNT + 12, 4, FL_O + tt * 16 + ct, ep, lane);
      if (w == 4) STAMP4(8);
      if (w == 4) { __builtin_amdgcn_s_sleep(OS); wait_flag<12>(A, FL_O + (st >> 1) * 16 + (lane & 15), ep);
        __hip_atomic_store(LDSP(unsigned, sm + CNT + 28), 1u, __ATOMIC_RELAXED, __HIP_MEMORY_SCOPE_WORKGROUP); }
    }
    BAR();
    v = red_get(v, sm, tg, role, lane);
    const float g0 = *LDSP(float, sm + GG + ((tg & 1) * 32 + r) * 4);
    v += rbf(1.f - g0) * (float)*LDSP(__bf16, sm + B2 + ((tg & 1) * 32 + r) * 2);
    *(bf8*)(A.out + (size_t)(st * 64 + (tg & 1) * 32 + r) * 1024 + (dt * 64 + (tg >> 1) * 32 + 16 * role + 8 * h) * 2) = __builtin_convertvector(v, bf8);
    if (FU && bid == 0 && tid == 0) st_flag(A, 0, ep);
    STAMP(11);
  }
}

extern "C" void kernel_launch(void* const* d_in, const int* in_sizes, int n_in, void* d_out, int out_size, void* d_ws,
                              size_t ws_size, hipStream_t stream) {
  Args A;
  A.x = (const char*)d_in[0]; A.wqkv = (const char*)d_in[1]; A.wphi = (const char*)d_in[2]; A.wo = (const char*)d_in[3];
  A.w1 = (const char*)d_in[5]; A.b1 = (const char*)d_in[6]; A.w2 = (const char*)d_in[7]; A.b2 = (const char*)d_in[8];
  A.gw = (const char*)d_in[9]; A.out = (char*)d_out; A.ws = (char*)d_ws;
  k_main<0><<<256, 512, 0, stream>>>(A);
}
```
